```python
import torch

def _softmax(x, axis=-1):
    return torch.softmax(x, dim=axis)
def _gelu(x):
    return torch.nn.functional.gelu(x, approximate="tanh")
def _silu(x):
    return torch.nn.functional.silu(x)
def _sigmoid(x):
    return torch.sigmoid(x)
def _layernorm(x, axis=-1, eps=1e-5):
    m = x.mean(dim=axis, keepdim=True)
    v = x.var(dim=axis, keepdim=True, unbiased=False)
    return (x - m) / torch.sqrt(v + eps)
def _scatter_add(tgt, idx, src, axis=0):
    return tgt.clone().index_add_(axis, idx, src)
def _segment_sum(src, seg, n):
    out = torch.zeros((n,) + src.shape[1:], dtype=src.dtype)
    return out.index_add_(0, seg, src)
def _pad(x, pw):
    flat = tuple(v for pair in reversed(pw) for v in pair)
    return torch.nn.functional.pad(x, flat)

kernel_name = 'concat_heads__ssm_conv_h3__linear_kernel_hedgehog__prompt'

def setup_inputs():

    _w = (lambda a: (lambda t: t.to(torch.bfloat16) if t.is_floating_point() else t)(torch.as_tensor(a)))
    def _A_setup():
        B, S, D = 1, 2048, 1024
        ks = 4
        return {
            "q": _w((lambda *s: torch.randn(*s, dtype=torch.bfloat16) * 0.05)(B, S, D)),
            "k": _w((lambda *s: torch.randn(*s, dtype=torch.bfloat16) * 0.8)(B, S, D)),
            "v": _w((lambda *s: torch.randn(*s, dtype=torch.bfloat16) * 0.8)(B, S, D)),
            "shift_k": _w((lambda *s: torch.randn(*s, dtype=torch.bfloat16) * 0.8)(D, ks) * 0.5),
            "ssm_k": _w((lambda *s: torch.randn(*s, dtype=torch.bfloat16) * 0.8)(D, S) * (1.0 / S ** 0.5)),
            "ssm_D": _w((lambda *s: torch.randn(*s, dtype=torch.bfloat16) * 0.8)(D)),
        }
    _A_in = _A_setup()

    def _B_setup():
        B, S, D, H = 1, 2048, 1024, 32
        Dh = D // H
        return {
            "x": _w((lambda *s: torch.randn(*s, dtype=torch.bfloat16) * 0.4)(B, S, D)),
            "wqkv": _w((lambda *s: torch.randn(*s, dtype=torch.bfloat16) * 0.4)(D, 3 * D)),
            "w_phi": _w((lambda *s: torch.randn(*s, dtype=torch.bfloat16) * 0.4)(H, Dh, Dh) * (Dh ** -0.5)),
            "wo": _w((lambda *s: torch.randn(*s, dtype=torch.bfloat16) * 0.05)(D, D)),
        }
    _B_in = _B_setup()

    return {
        "A_q": _A_in["q"],
        "A_k": _A_in["k"],
        "A_v": _A_in["v"],
        "A_shift_k": _A_in["shift_k"],
        "A_ssm_k": _A_in["ssm_k"],
        "A_ssm_D": _A_in["ssm_D"],
        "B_x": _B_in["x"],
        "B_wqkv": _B_in["wqkv"],
        "B_w_phi": _B_in["w_phi"],
        "B_wo": _B_in["wo"],
    }

def reference(A_q, A_k, A_v, A_shift_k, A_ssm_k, A_ssm_D, B_x, B_wqkv, B_w_phi, B_wo):
    def _A_ref(q, k, v, shift_k, ssm_k, ssm_D):
        B, S, D = q.shape
        ks = shift_k.shape[-1]
        k_pad = _pad(k, ((0, 0), (ks - 1, 0), (0, 0)))
        k_shift = torch.zeros_like(k)
        for t in range(ks):
            k_shift = k_shift + k_pad[:, t:t + S, :] * shift_k[:, ks - 1 - t][None, None, :]
        kv = k_shift * v
        L = 2 * S
        kv_pad = _pad(kv, ((0, 0), (0, S), (0, 0)))
        kf_pad = _pad(ssm_k, ((0, 0), (0, S)))
        kv_f = (lambda x, axes=(-2,-1): torch.fft.rfft2(x.float(), dim=axes))(kv_pad[:, None, :, :], axes=(1, 2))
        kf_f = (lambda x, axes=(-2,-1): torch.fft.rfft2(x.float(), dim=axes))(kf_pad[:, None, :], axes=(1, 2))
        kf_f = (lambda x, *d: x.permute(*d))(kf_f, 1, 2, 0)
        y_f = kv_f * kf_f[None, :, :, :]
        y = (lambda x, s, axes=(-2,-1): torch.fft.irfft2(x, s=s, dim=axes).to(torch.bfloat16))(y_f, (1, L), axes=(1, 2))[:, 0, :S, :]
        y = y + kv * ssm_D[None, None, :]
        return q * y

    def _B_ref(x, wqkv, w_phi, wo):
        B, S, D = x.shape
        H = 32; Dh = D // H
        qkv = (lambda x, *d: x.permute(*d))((x @ wqkv).reshape(B, S, 3, H, Dh), 2, 0, 3, 1, 4)
        q, k, v = qkv[0], qkv[1], qkv[2]
        pq = torch.einsum("bhsd,hdf->bhsf", q, w_phi)
        pk = torch.einsum("bhsd,hdf->bhsf", k, w_phi)
        phi_q = torch.concatenate([_softmax(pq, axis=-1), _softmax(-pq, axis=-1)], axis=-1)
        phi_k = torch.concatenate([_softmax(pk, axis=-1), _softmax(-pk, axis=-1)], axis=-1)
        kv = torch.einsum("bhsf,bhsd->bhfd", phi_k, v)
        z  = torch.einsum("bhsf->bhf", phi_k)
        num = torch.einsum("bhqf,bhfd->bhqd", phi_q, kv)
        den = torch.einsum("bhqf,bhf->bhq", phi_q, z)[..., None] + 1e-6
        o = num / den
        return (lambda x, *d: x.permute(*d))(o, 0, 2, 1, 3).reshape(B, S, D) @ wo

    a = _A_ref(A_q, A_k, A_v, A_shift_k, A_ssm_k, A_ssm_D)
    b = _B_ref(B_x, B_wqkv, B_w_phi, B_wo)
    return torch.concatenate([a, b], axis=-1)
```

```cpp
#include <hip/hip_runtime.h>
#include <hip/hip_bf16.h>
#include <stdint.h>

typedef unsigned short u16;
__device__ __forceinline__ float bf2f(u16 x){ return __uint_as_float(((unsigned)x)<<16); }
__device__ __forceinline__ u16 f2bf(float f){ unsigned u=__float_as_uint(f); u += 0x7fffu + ((u>>16)&1u); return (u16)(u>>16); }
__device__ __forceinline__ float rbf(float f){ return bf2f(f2bf(f)); }

#define S_ 2048
#define D_ 1024

__global__ void n_kv(const u16* k, const u16* v, const u16* sk, u16* kv){
  int idx = blockIdx.x*blockDim.x+threadIdx.x; int s = idx / D_, d = idx % D_;
  float acc = 0.f;
  for(int t=0;t<4;t++){ int ss = s + t - 3; float kk = ss>=0 ? bf2f(k[ss*D_+d]) : 0.f; float w = bf2f(sk[d*4 + 3 - t]); acc = rbf(acc + rbf(kk*w)); }
  kv[idx] = f2bf(acc * bf2f(v[idx]));
}
__global__ void n_conv(const u16* kv, const u16* h, const u16* Dv, const u16* q, u16* out){
  int idx = blockIdx.x*blockDim.x+threadIdx.x; int s = idx / D_, d = idx % D_;
  float acc = 0.f;
  for(int j=0;j<=s;j++) acc += bf2f(h[d*S_+j]) * bf2f(kv[(s-j)*D_+d]);
  float y = rbf(acc);
  y = rbf(y + rbf(bf2f(kv[idx])*bf2f(Dv[d])));
  out[s*2048 + d] = f2bf(bf2f(q[idx])*y);
}
__global__ void n_gemm(const u16* A, const u16* B, u16* C, int M, int N, int K, int ldc){
  __shared__ float sa[16][17], sb[16][17];
  int tx=threadIdx.x, ty=threadIdx.y; int row=blockIdx.y*16+ty, col=blockIdx.x*16+tx; float acc=0;
  for(int k0=0;k0<K;k0+=16){ sa[ty][tx]=bf2f(A[row*K+k0+tx]); sb[ty][tx]=bf2f(B[(k0+ty)*N+col]); __syncthreads();
    for(int k=0;k<16;k++) acc+=sa[ty][k]*sb[k][tx]; __syncthreads(); }
  C[row*ldc+col]=f2bf(acc);
}
__global__ void n_phi(const u16* qkv, const u16* wphi, u16* phi  ){
  int idx = blockIdx.x*blockDim.x+threadIdx.x;
  int which = idx / (S_*32); int r = idx % (S_*32); int s = r/32, h = r%32;
  float p[32];
  for(int f=0;f<32;f++){ float a=0; for(int d=0;d<32;d++) a += bf2f(qkv[s*3072+which*1024+h*32+d])*bf2f(wphi[h*1024+d*32+f]); p[f]=rbf(a); }
  float mx=-1e30f, mn=1e30f; for(int f=0;f<32;f++){ mx=fmaxf(mx,p[f]); mn=fminf(mn,p[f]); }
  float s1=0,s2=0; for(int f=0;f<32;f++){ s1+=expf(p[f]-mx); s2+=expf(mn-p[f]); }
  u16* o = phi + ((size_t)(which*S_+s)*32+h)*64;
  for(int f=0;f<32;f++){ o[f]=f2bf(expf(p[f]-mx)/s1); o[32+f]=f2bf(expf(mn-p[f])/s2); }
}
__global__ void n_kvz(const u16* phik, const u16* qkv, u16* kvm  ){
  int idx = blockIdx.x*blockDim.x+threadIdx.x; if(idx>=32*64*33) return; int h=idx/(64*33); int r=idx%(64*33); int f=r/33, d=r%33;
  float a=0; for(int s=0;s<S_;s++){ float p=bf2f(phik[((size_t)s*32+h)*64+f]); a += d<32 ? p*bf2f(qkv[s*3072+2048+h*32+d]) : p; }
  kvm[idx]=f2bf(a);
}
__global__ void n_o(const u16* phiq, const u16* kvm, u16* o){
  int idx = blockIdx.x*blockDim.x+threadIdx.x; int s=idx/1024, c=idx%1024, h=c/32, d=c%32;
  float n=0, de=0; for(int f=0;f<64;f++){ float p=bf2f(phiq[((size_t)s*32+h)*64+f]); n+=p*bf2f(kvm[(h*64+f)*33+d]); de+=p*bf2f(kvm[(h*64+f)*33+32]); }
  n=rbf(n); de=rbf(rbf(de)+1e-6f); o[idx]=f2bf(n/de);
}

extern "C" void kernel_launch(void* const* d_in, const int* in_sizes, int n_in,
                              void* d_out, int out_size, void* d_ws, size_t ws_size,
                              hipStream_t stream) {
  const u16* q=(const u16*)d_in[0]; const u16* k=(const u16*)d_in[1]; const u16* v=(const u16*)d_in[2];
  const u16* sk=(const u16*)d_in[3]; const u16* h=(const u16*)d_in[4]; const u16* Dv=(const u16*)d_in[5];
  const u16* x=(const u16*)d_in[6]; const u16* wqkv=(const u16*)d_in[7]; const u16* wphi=(const u16*)d_in[8]; const u16* wo=(const u16*)d_in[9];
  u16* out=(u16*)d_out; char* ws=(char*)d_ws;
  u16* kv=(u16*)ws; u16* qkv=(u16*)(ws+(4<<20)); u16* phi=(u16*)(ws+(20<<20)); u16* kvm=(u16*)(ws+(40<<20)); u16* o=(u16*)(ws+(44<<20));
  n_kv<<<S_*D_/256,256,0,stream>>>(k,v,sk,kv);
  n_conv<<<S_*D_/256,256,0,stream>>>(kv,h,Dv,q,out);
  n_gemm<<<dim3(3072/16,2048/16),dim3(16,16),0,stream>>>(x,wqkv,qkv,2048,3072,1024,3072);
  n_phi<<<2*S_*32/256,256,0,stream>>>(qkv,wphi,phi);
  n_kvz<<<(32*64*33+255)/256,256,0,stream>>>(phi+(size_t)S_*32*64,qkv,kvm);
  n_o<<<S_*1024/256,256,0,stream>>>(phi,kvm,o);
  n_gemm<<<dim3(1024/16,2048/16),dim3(16,16),0,stream>>>(o,wo,out+1024,2048,1024,1024,2048);
}
```

```cpp
#include <hip/hip_runtime.h>
#include <hip/hip_bf16.h>
#include <stdint.h>

#define LAS __attribute__((address_space(3)))
#define GAS __attribute__((address_space(1)))
#define DI __device__ __forceinline__
typedef unsigned short u16;
typedef __bf16 bf16x8 __attribute__((ext_vector_type(8)));
typedef __bf16 bf16x4 __attribute__((ext_vector_type(4)));
typedef __bf16 bf16x2 __attribute__((ext_vector_type(2)));
typedef float f32x16 __attribute__((ext_vector_type(16)));
typedef float f32x4 __attribute__((ext_vector_type(4)));
typedef unsigned u32x4 __attribute__((ext_vector_type(4)));
typedef unsigned u32x2 __attribute__((ext_vector_type(2)));

constexpr size_t WS_PHIQ = 0;
constexpr size_t WS_PART = 8u << 20;
constexpr size_t WS_ZPART = 12u << 20;
constexpr size_t WS_UR = 16u << 20;
constexpr size_t WS_Y2T = 24u << 20;
constexpr size_t WS_KVF = 32u << 20;
constexpr size_t WS_ZF = 33u << 20;
constexpr size_t WS_OF = 40u << 20;
constexpr size_t WS_STAMP = 64u << 20;
constexpr int UROW = 4128;

constexpr int STG = 40960;
constexpr int K1_LDS = 3 * STG;

#define MFMA32(a, b, c) __builtin_amdgcn_mfma_f32_32x32x16_bf16((a), (b), (c), 0, 0, 0)
#define MFMA16(a, b, c) __builtin_amdgcn_mfma_f32_16x16x32_bf16((a), (b), (c), 0, 0, 0)
#define WAITV(n) asm volatile("s_waitcnt vmcnt(" #n ")" ::: "memory")
#define WAITL(n) asm volatile("s_waitcnt lgkmcnt(" #n ")" ::: "memory")
#define BAR() __builtin_amdgcn_s_barrier()
#define SCHED() __builtin_amdgcn_sched_barrier(0)

DI bf16x8 trfrag(unsigned a0, unsigned d) {
  bf16x4 lo = __builtin_amdgcn_ds_read_tr16_b64_v4bf16((LAS bf16x4*)(a0));
  bf16x4 hi = __builtin_amdgcn_ds_read_tr16_b64_v4bf16((LAS bf16x4*)(a0 + d));
  return __builtin_shufflevector(lo, hi, 0, 1, 2, 3, 4, 5, 6, 7);
}
DI bf16x8 pack8(const f32x16& x, int s) {
  bf16x8 r;
#pragma unroll
  for (int j = 0; j < 8; ++j) r[j] = (__bf16)x[8 * s + j];
  return r;
}
DI float rbf(float x) { return (float)(__bf16)x; }
DI float blo(unsigned x) { return __uint_as_float(x << 16); }
DI float bhi(unsigned x) { return __uint_as_float(x & 0xffff0000u); }
DI unsigned pk2(float a, float b) { bf16x2 v = {(__bf16)a, (__bf16)b}; return __builtin_bit_cast(unsigned, v); }
DI float xhalf_max(float x) { return fmaxf(x, __shfl_xor(x, 32)); }
DI float xhalf_min(float x) { return fminf(x, __shfl_xor(x, 32)); }
DI float xhalf_sum(float x) { return x + __shfl_xor(x, 32); }

__global__ __launch_bounds__(512, 2) void k1(const u16* __restrict__ X, const u16* __restrict__ W, const u16* __restrict__ wphi, char* __restrict__ ws) {
  extern __shared__ __attribute__((aligned(16))) char sm[];
  const int wave = __builtin_amdgcn_readfirstlane(threadIdx.x >> 6);
  const int lane = threadIdx.x & 63;
  const int xcd = blockIdx.x & 7, idx = blockIdx.x >> 3;
  const int rb = (xcd & 1) * 8 + (idx & 7), hp = (xcd >> 1) * 4 + (idx >> 3);
  const unsigned smb = (unsigned)(size_t)sm;
  const int rot = (2 * (idx & 7) + 4 * (idx >> 3) + (idx >> 4)) & 15;
  if (wave >= 4) {
    const int lw = wave - 4;
    const int lr = lane >> 3, lc = lane & 7;
    const char* xg[4]; const char* wg[6];
#pragma unroll
    for (int i = 0; i < 4; ++i) { int r = 32 * lw + 8 * i + lr; xg[i] = (const char*)X + (size_t)(rb * 128 + r) * 2048 + ((lc ^ ((r >> 1) & 7)) << 4); }
#pragma unroll
    for (int j = 0; j < 6; ++j) { int id = lw * 6 + j, seg = id >> 3, kb = id & 7, kr = 8 * kb + lr; wg[j] = (const char*)W + ((size_t)kr * 3072 + seg * 1024 + hp * 64) * 2 + ((lc ^ (((kr >> 1) & 1) << 2)) << 4); }
    auto issue = [&](int t, int slot) {
      const int kt = (t + rot) & 15;
      char* sb = sm + slot * STG;
#pragma unroll
      for (int i = 0; i < 4; ++i) __builtin_amdgcn_global_load_lds((const GAS unsigned*)(xg[i] + kt * 128), (LAS unsigned*)(sb + (32 * lw + 8 * i) * 128), 16, 0, 0);
#pragma unroll
      for (int j = 0; j < 6; ++j) __builtin_amdgcn_global_load_lds((const GAS unsigned*)(wg[j] + (size_t)kt * 64 * 6144), (LAS unsigned*)(sb + 16384 + (lw * 6 + j) * 1024), 16, 0, 0);
    };
    issue(0, 0); issue(1, 1);
    WAITV(10); BAR();
    int slot = 2;
    for (int t = 0; t < 14; ++t) {
      issue(t + 2, slot); slot = slot == 2 ? 0 : slot + 1;
      WAITV(10); BAR();
    }
    WAITV(0); BAR();
    return;
  }
  const int mi = wave & 1, hi = wave >> 1, head = hp * 2 + hi;
  const int h = lane >> 5, l31 = lane & 31, blk = (lane >> 4) & 1, q4 = (lane & 15) >> 2, p4 = lane & 3;
  bf16x8 wf[2];
#pragma unroll
  for (int s = 0; s < 2; ++s)
#pragma unroll
    for (int j = 0; j < 8; ++j) { int d = 16 * s + 8 * (j >> 2) + 4 * h + (j & 3); wf[s][j] = __builtin_bit_cast(__bf16, wphi[head * 1024 + d * 32 + l31]); }
  unsigned xa[4];
  { int r = 64 * mi + l31;
#pragma unroll
    for (int s = 0; s < 4; ++s) xa[s] = smb + r * 128 + (((2 * s + h) ^ ((r >> 1) & 7)) << 4); }
  const unsigned wa = smb + 16384 + (8 * h + q4) * 128 + (((4 * hi + 2 * blk + (p4 >> 1)) ^ ((q4 >> 1) << 2)) << 4) + 8 * (p4 & 1);
  f32x16 acc[3][2];
#pragma unroll
  for (int c = 0; c < 3; ++c) { acc[c][0] = (f32x16){}; acc[c][1] = (f32x16){}; }
  bf16x8 fx[2][2], fw[2][3];
  auto ld = [&](int b, unsigned so, int s) {
#pragma unroll
    for (int c = 0; c < 3; ++c) fw[b][c] = trfrag(wa + so + c * 8192 + s * 2048, 512);
#pragma unroll
    for (int tt = 0; tt < 2; ++tt) fx[b][tt] = *(const LAS bf16x8*)(xa[s] + so + tt * 4096);
  };
  auto mm = [&](int b) {
#pragma unroll
    for (int c = 0; c < 3; ++c)
#pragma unroll
      for (int tt = 0; tt < 2; ++tt) acc[c][tt] = MFMA32(fw[b][c], fx[b][tt], acc[c][tt]);
  };
  BAR();
  ld(0, 0, 0);
  unsigned so = 0;
  for (int t = 0; t < 16; ++t) {
    ld(1, so, 1); SCHED(); mm(0); SCHED();
    ld(0, so, 2); SCHED(); mm(1); SCHED();
    ld(1, so, 3); SCHED(); mm(0); SCHED();
    so = so == 2 * STG ? 0 : so + STG;
    if (t < 15) { WAITL(0); BAR(); ld(0, so, 0); }
    SCHED(); mm(1); SCHED();
  }
  constexpr float L2E = 1.4426950408889634f;
  const unsigned pk_img = smb + STG + hi * 16384;
  const unsigned v_img = smb + STG + 32768 + hi * 8192;
#pragma unroll
  for (int c = 0; c < 2; ++c)
#pragma unroll
    for (int tt = 0; tt < 2; ++tt) {
      f32x16 p = (f32x16){};
      p = MFMA32(wf[0], pack8(acc[c][tt], 0), p);
      p = MFMA32(wf[1], pack8(acc[c][tt], 1), p);
      float mx = -3.0e38f, mn = 3.0e38f;
#pragma unroll
      for (int i = 0; i < 16; ++i) { p[i] = rbf(p[i]); mx = fmaxf(mx, p[i]); mn = fminf(mn, p[i]); }
      mx = xhalf_max(mx); mn = xhalf_min(mn);
      f32x16 e1, e2; float s1 = 0.f, s2 = 0.f;
#pragma unroll
      for (int i = 0; i < 16; ++i) { e1[i] = __builtin_amdgcn_exp2f((p[i] - mx) * L2E); e2[i] = __builtin_amdgcn_exp2f((mn - p[i]) * L2E); s1 += e1[i]; s2 += e2[i]; }
      s1 = __builtin_amdgcn_rcpf(xhalf_sum(s1)); s2 = __builtin_amdgcn_rcpf(xhalf_sum(s2));
#pragma unroll
      for (int i = 0; i < 16; ++i) { e1[i] *= s1; e2[i] *= s2; }
      bf16x8 f0 = pack8(e1, 0), f1 = pack8(e1, 1), f2 = pack8(e2, 0), f3 = pack8(e2, 1);
      if (c == 0) {
        char* dst = ws + WS_PHIQ + ((size_t)((rb * 4 + mi * 2 + tt) * 32 + head) * 4) * 1024 + lane * 16;
        *(bf16x8*)(dst) = f0; *(bf16x8*)(dst + 1024) = f1; *(bf16x8*)(dst + 2048) = f2; *(bf16x8*)(dst + 3072) = f3;
      } else {
        const int n = 64 * mi + 32 * tt + l31;
        const int g = (n & 1) | (((n >> 2) & 3) << 1) | (((n >> 1) & 1) << 3);
        const unsigned rowa = pk_img + n * 128;
        auto wr = [&](int u, bf16x4 v) { *(LAS bf16x4*)(rowa + ((u ^ g) << 3)) = v; };
        wr(0 + h, __builtin_shufflevector(f0, f0, 0, 1, 2, 3)); wr(2 + h, __builtin_shufflevector(f0, f0, 4, 5, 6, 7));
        wr(4 + h, __builtin_shufflevector(f1, f1, 0, 1, 2, 3)); wr(6 + h, __builtin_shufflevector(f1, f1, 4, 5, 6, 7));
        wr(8 + h, __builtin_shufflevector(f2, f2, 0, 1, 2, 3)); wr(10 + h, __builtin_shufflevector(f2, f2, 4, 5, 6, 7));
        wr(12 + h, __builtin_shufflevector(f3, f3, 0, 1, 2, 3)); wr(14 + h, __builtin_shufflevector(f3, f3, 4, 5, 6, 7));
      }
    }
#pragma unroll
  for (int tt = 0; tt < 2; ++tt) {
    const int n = 64 * mi + 32 * tt + l31;
    const int g = (n >> 1) & 7;
    const unsigned rowa = v_img + n * 64;
    bf16x8 f0 = pack8(acc[2][tt], 0), f1 = pack8(acc[2][tt], 1);
    *(LAS bf16x4*)(rowa + (((0 + h) ^ g) << 3)) = __builtin_shufflevector(f0, f0, 0, 1, 2, 3);
    *(LAS bf16x4*)(rowa + (((2 + h) ^ g) << 3)) = __builtin_shufflevector(f0, f0, 4, 5, 6, 7);
    *(LAS bf16x4*)(rowa + (((4 + h) ^ g) << 3)) = __builtin_shufflevector(f1, f1, 0, 1, 2, 3);
    *(LAS bf16x4*)(rowa + (((6 + h) ^ g) << 3)) = __builtin_shufflevector(f1, f1, 4, 5, 6, 7);
  }
  WAITL(0); BAR();
  f32x16 dkv = (f32x16){}, dz = (f32x16){};
  bf16x8 ones;
#pragma unroll
  for (int j = 0; j < 8; ++j) ones[j] = (__bf16)1.0f;
#pragma unroll
  for (int ks = 0; ks < 8; ++ks) {
    bf16x4 a[2], b[2];
#pragma unroll
    for (int t = 0; t < 2; ++t) {
      const int n = 16 * ks + 8 * h + 4 * t + q4;
      const int g = (n & 1) | (((n >> 2) & 3) << 1) | (((n >> 1) & 1) << 3);
      a[t] = __builtin_amdgcn_ds_read_tr16_b64_v4bf16((LAS bf16x4*)(pk_img + n * 128 + (((8 * mi + 4 * blk + p4) ^ g) << 3)));
      b[t] = __builtin_amdgcn_ds_read_tr16_b64_v4bf16((LAS bf16x4*)(v_img + n * 64 + (((4 * blk + p4) ^ ((n >> 1) & 7)) << 3)));
    }
    bf16x8 af = __builtin_shufflevector(a[0], a[1], 0, 1, 2, 3, 4, 5, 6, 7), bfr = __builtin_shufflevector(b[0], b[1], 0, 1, 2, 3, 4, 5, 6, 7);
    dkv = MFMA32(af, bfr, dkv);
    dz = MFMA32(af, ones, dz);
  }
  {
    char* dst = ws + WS_PART + ((size_t)(head * 16 + rb) * 2 + mi) * 4096 + lane * 16;
#pragma unroll
    for (int a = 0; a < 4; ++a) *(f32x4*)(dst + a * 1024) = (f32x4){dkv[4 * a], dkv[4 * a + 1], dkv[4 * a + 2], dkv[4 * a + 3]};
    if (l31 == 0) {
      char* zd = ws + WS_ZPART + (((size_t)(head * 16 + rb) * 2 + mi) * 2 + h) * 64;
#pragma unroll
      for (int a = 0; a < 4; ++a) *(f32x4*)(zd + a * 16) = (f32x4){dz[4 * a], dz[4 * a + 1], dz[4 * a + 2], dz[4 * a + 3]};
    }
  }
}

__global__ __launch_bounds__(256) void kred(char* __restrict__ ws) {
  const int head = blockIdx.x, w = threadIdx.x >> 6, lane = threadIdx.x & 63;
  const int T = w >> 1, s = w & 1;
  f32x4 a0 = {}, a1 = {}, z0 = {}, z1 = {};
#pragma unroll
  for (int rb = 0; rb < 16; ++rb) {
    const char* src = ws + WS_PART + ((size_t)(head * 16 + rb) * 2 + T) * 4096 + (2 * s) * 1024 + lane * 16;
    a0 += *(const f32x4*)src; a1 += *(const f32x4*)(src + 1024);
    const char* zs = ws + WS_ZPART + (((size_t)(head * 16 + rb) * 2 + T) * 2 + (lane >> 5)) * 64 + s * 32;
    z0 += *(const f32x4*)zs; z1 += *(const f32x4*)(zs + 16);
  }
  u32x4 r = {pk2(a0[0], a0[1]), pk2(a0[2], a0[3]), pk2(a1[0], a1[1]), pk2(a1[2], a1[3])};
  *(u32x4*)(ws + WS_KVF + ((size_t)head * 4 + w) * 1024 + lane * 16) = r;
  u32x4 rz = {pk2(z0[0], z0[1]), pk2(z0[2], z0[3]), pk2(z1[0], z1[1]), pk2(z1[2], z1[3])};
  if ((lane & 31) == 0) *(u32x4*)(ws + WS_ZF + (((size_t)head * 4 + w) * 2 + (lane >> 5)) * 16) = rz;
}

__global__ __launch_bounds__(256) void ko(char* __restrict__ ws) {
  const int gw = blockIdx.x * 4 + (threadIdx.x >> 6), lane = threadIdx.x & 63;
  const int head = gw & 31, tile = gw >> 5;
  f32x16 num = {}, den = {};
#pragma unroll
  for (int f = 0; f < 4; ++f) {
    bf16x8 a = *(const bf16x8*)(ws + WS_KVF + ((size_t)head * 4 + f) * 1024 + lane * 16);
    bf16x8 z = *(const bf16x8*)(ws + WS_ZF + (((size_t)head * 4 + f) * 2 + (lane >> 5)) * 16);
    bf16x8 b = *(const bf16x8*)(ws + WS_PHIQ + ((size_t)(tile * 32 + head) * 4 + f) * 1024 + lane * 16);
    num = MFMA32(a, b, num); den = MFMA32(z, b, den);
  }
  const float dn = rbf(rbf(den[0]) + 1e-6f);
  const float rd = 1.0f / dn;
#pragma unroll
  for (int i = 0; i < 16; ++i) num[i] = rbf(num[i]) * rd;
  char* dst = ws + WS_OF + ((size_t)(tile * 32 + head) * 2) * 1024 + lane * 16;
  *(bf16x8*)dst = pack8(num, 0); *(bf16x8*)(dst + 1024) = pack8(num, 1);
}

constexpr int K3_LDS = 131072;
__global__ __launch_bounds__(512, 2) void kg2(const u16* __restrict__ wo, const char* __restrict__ ws, u16* __restrict__ out) {
  extern __shared__ __attribute__((aligned(16))) char sm[];
  const int wave = __builtin_amdgcn_readfirstlane(threadIdx.x >> 6);
  const int lane = threadIdx.x & 63;
  const int xcd = blockIdx.x & 7, idx = blockIdx.x >> 3;
  const int rb = (xcd >> 1) * 4 + (idx >> 3), cb = (xcd & 1) * 8 + (idx & 7);
  const unsigned smb = (unsigned)(size_t)sm;
  {
    const int lr = lane >> 3, lc = lane & 7;
#pragma unroll
    for (int i = 0; i < 16; ++i) {
      const int pc = ((wave * 16 + i) + (idx >> 3) * 32) & 127, kr = pc * 8 + lr;
      __builtin_amdgcn_global_load_lds((const GAS unsigned*)((const char*)wo + (size_t)kr * 2048 + cb * 128 + ((lc ^ (((kr >> 1) & 1) << 2)) << 4)), (LAS unsigned*)(sm + pc * 1024), 16, 0, 2);
    }
  }
  const int tt = wave & 3, kh = wave >> 2;
  const int h = lane >> 5, l31 = lane & 31, blk = (lane >> 4) & 1, q4 = (lane & 15) >> 2, p4 = lane & 3;
  const int hrot = (idx & 7) * 2;
  bf16x8 of[32];
  const char* osrc = ws + WS_OF + ((size_t)((rb * 4 + tt) * 32 + kh * 16) * 2) * 1024 + lane * 16;
#pragma unroll
  for (int i = 0; i < 16; ++i) { const int hh = (i + hrot) & 15; of[2 * i] = *(const bf16x8*)(osrc + hh * 2048); of[2 * i + 1] = *(const bf16x8*)(osrc + hh * 2048 + 1024); }
  WAITV(0);
  __syncthreads();
  f32x16 acc[2] = {(f32x16){}, (f32x16){}};
  const unsigned wa0 = smb + (kh * 512 + 4 * h + q4) * 128 + (((2 * blk + (p4 >> 1)) ^ ((q4 >> 1) << 2)) << 4) + 8 * (p4 & 1);
  const unsigned wa1 = smb + (kh * 512 + 4 * h + q4) * 128 + (((4 + 2 * blk + (p4 >> 1)) ^ ((q4 >> 1) << 2)) << 4) + 8 * (p4 & 1);
#pragma unroll
  for (int i = 0; i < 16; ++i) {
    const unsigned ho = ((i + hrot) & 15) * 4096;
#pragma unroll
    for (int s = 0; s < 2; ++s) {
      bf16x8 a0 = trfrag(wa0 + ho + s * 2048, 1024), a1 = trfrag(wa1 + ho + s * 2048, 1024);
      acc[0] = MFMA32(a0, of[2 * i + s], acc[0]); acc[1] = MFMA32(a1, of[2 * i + s], acc[1]);
    }
  }
  __syncthreads();
  if (kh == 1) {
#pragma unroll
    for (int c = 0; c < 2; ++c)
#pragma unroll
      for (int a = 0; a < 4; ++a) *(LAS f32x4*)(smb + ((tt * 8 + c * 4 + a) * 64 + lane) * 16) = (f32x4){acc[c][4 * a], acc[c][4 * a + 1], acc[c][4 * a + 2], acc[c][4 * a + 3]};
  }
  __syncthreads();
  if (kh == 0) {
    u16* orow = out + (size_t)(rb * 128 + tt * 32 + l31) * 2048 + 1024 + cb * 64;
#pragma unroll
    for (int c = 0; c < 2; ++c)
#pragma unroll
      for (int a = 0; a < 4; ++a) {
        f32x4 o = *(const LAS f32x4*)(smb + ((tt * 8 + c * 4 + a) * 64 + lane) * 16);
        u32x2 r = {pk2(acc[c][4 * a] + o[0], acc[c][4 * a + 1] + o[1]), pk2(acc[c][4 * a + 2] + o[2], acc[c][4 * a + 3] + o[3])};
        *(u32x2*)(orow + c * 32 + 8 * a + 4 * h) = r;
      }
  }
}

__global__ __launch_bounds__(256) void ka1(const u16* __restrict__ K, const u16* __restrict__ V, const u16* __restrict__ SK, char* __restrict__ ws) {
  __shared__ __attribute__((aligned(16))) u16 tile[64 * 136];
  const int xcd = blockIdx.x & 7, idx = blockIdx.x >> 3;
  const int cg = xcd * 2 + (idx & 1), sb = idx >> 1;
  const int tid = threadIdx.x, c8 = tid & 7, r4 = tid >> 3;
  const int ch0 = cg * 64 + c8 * 8, s0 = sb * 128 + r4 * 4;
  u32x4 kr[7], vr[4];
#pragma unroll
  for (int i = 0; i < 7; ++i) { const int s = s0 - 3 + i; kr[i] = s >= 0 ? *(const u32x4*)(K + (size_t)s * 1024 + ch0) : (u32x4){0, 0, 0, 0}; }
#pragma unroll
  for (int i = 0; i < 4; ++i) vr[i] = *(const u32x4*)(V + (size_t)(s0 + i) * 1024 + ch0);
  u32x4 wv[4];
#pragma unroll
  for (int i = 0; i < 4; ++i) wv[i] = *(const u32x4*)(SK + (size_t)ch0 * 4 + i * 8);
#pragma unroll
  for (int c = 0; c < 8; ++c) {
    const unsigned w01 = wv[c >> 1][(c & 1) * 2], w23 = wv[c >> 1][(c & 1) * 2 + 1];
    const float w0 = blo(w01), w1 = bhi(w01), w2 = blo(w23), w3 = bhi(w23);
    float kk[7], u[4];
#pragma unroll
    for (int i = 0; i < 7; ++i) kk[i] = (c & 1) ? bhi(kr[i][c >> 1]) : blo(kr[i][c >> 1]);
#pragma unroll
    for (int r = 0; r < 4; ++r) {
      float a = rbf(kk[r] * w3);
      a = rbf(a + rbf(kk[r + 1] * w2));
      a = rbf(a + rbf(kk[r + 2] * w1));
      a = rbf(a + rbf(kk[r + 3] * w0));
      u[r] = a * ((c & 1) ? bhi(vr[r][c >> 1]) : blo(vr[r][c >> 1]));
    }
    u32x2 pr = {pk2(u[3], u[2]), pk2(u[1], u[0])};
    *(u32x2*)(tile + (c8 * 8 + c) * 136 + 124 - 4 * r4) = pr;
  }
  __syncthreads();
#pragma unroll
  for (int it = 0; it < 4; ++it) {
    const int e = it * 256 + tid, ch = e >> 4, c16 = e & 15;
    u32x4 v = *(const u32x4*)(tile + ch * 136 + c16 * 8);
    *(u32x4*)(ws + WS_UR + (size_t)(cg * 64 + ch) * UROW + (1920 - 128 * sb) * 2 + c16 * 16) = v;
  }
  if (sb == 0 && tid < 128) { *(u32x4*)(ws + WS_UR + (size_t)(cg * 64 + (tid >> 1)) * UROW + 4096 + (tid & 1) * 16) = (u32x4){0, 0, 0, 0}; }
}

constexpr int CV_H = 0;
constexpr int CV_U0 = 4608;
constexpr int CV_U1 = 4608 + 4160 + 64;
constexpr int CV_CH = 13056;
__global__ __launch_bounds__(256) void kconv(const u16* __restrict__ H, const u16* __restrict__ Dv, char* __restrict__ ws) {
  __shared__ __attribute__((aligned(16))) char sm[4 * CV_CH];
  const int wave = __builtin_amdgcn_readfirstlane(threadIdx.x >> 6), lane = threadIdx.x & 63;
  const int xcd = blockIdx.x & 7, idx = blockIdx.x >> 3;
  const int ch = xcd * 128 + idx * 4 + wave;
  char* cs = sm + wave * CV_CH;
  const unsigned cb = (unsigned)(size_t)cs;
  const char* ur = ws + WS_UR + (size_t)ch * UROW;
  u32x4 hv[4], uv[5]; unsigned un[5];
#pragma unroll
  for (int i = 0; i < 4; ++i) hv[i] = *(const u32x4*)((const char*)H + (size_t)ch * 4096 + i * 1024 + lane * 16);
#pragma unroll
  for (int i = 0; i < 4; ++i) { uv[i] = *(const u32x4*)(ur + i * 1024 + lane * 16); un[i] = *(const unsigned*)(ur + i * 1024 + lane * 16 + 16); }
  if (lane < 32) *(LAS u32x4*)(cb + CV_H + lane * 16) = (u32x4){0, 0, 0, 0};
  if (lane < 2) { *(LAS u32x4*)(cb + CV_U0 + 4096 + lane * 16) = (u32x4){0, 0, 0, 0}; *(LAS u32x4*)(cb + CV_U1 + 4096 + lane * 16) = (u32x4){0, 0, 0, 0}; }
#pragma unroll
  for (int i = 0; i < 4; ++i) {
    *(LAS u32x4*)(cb + CV_H + 512 + i * 1024 + lane * 16) = hv[i];
    *(LAS u32x4*)(cb + CV_U0 + i * 1024 + lane * 16) = uv[i];
    u32x4 sh = {__builtin_amdgcn_alignbit(uv[i][1], uv[i][0], 16), __builtin_amdgcn_alignbit(uv[i][2], uv[i][1], 16), __builtin_amdgcn_alignbit(uv[i][3], uv[i][2], 16), __builtin_amdgcn_alignbit(un[i], uv[i][3], 16)};
    *(LAS u32x4*)(cb + CV_U1 + i * 1024 + lane * 16) = sh;
  }
  WAITL(0);
  __builtin_amdgcn_wave_barrier();
  const int m = lane & 15, g = lane >> 4;
  const int st0 = 2055 - 8 * g - m;
  const unsigned aa = (m & 1) ? cb + CV_U0 + st0 * 2 : cb + CV_U1 + (st0 - 1) * 2;
  const unsigned ba = cb + CV_H + 512 + (16 * m + 8 - 8 * g) * 2;
  f32x4 acc[8];
#pragma unroll
  for (int i = 0; i < 8; ++i) acc[i] = (f32x4){};
  for (int r = 0; r < 8; ++r) {
    bf16x8 A[8], B[8];
    const unsigned ar = aa - 64 * r, br = ba - 64 * r;
#pragma unroll
    for (int p = 0; p < 8; ++p) {
      const LAS unsigned* pa = (const LAS unsigned*)(ar - 512 * p);
      u32x4 t = {pa[0], pa[1], pa[2], pa[3]};
      A[p] = __builtin_bit_cast(bf16x8, t);
      B[p] = *(const LAS bf16x8*)(br + 512 * p);
    }
#pragma unroll
    for (int i = 0; i < 8; ++i)
#pragma unroll
      for (int p = 0; p <= i; ++p) acc[i] = MFMA16(A[p], B[i - p], acc[i]);
  }
  const float dd = blo((unsigned)Dv[ch]);
#pragma unroll
  for (int i = 0; i < 8; ++i) {
    const int s = 256 * i + 16 * m + 4 * g;
    u32x2 uu = *(const LAS u32x2*)(cb + CV_U0 + (2044 - s) * 2);
    const float y0 = rbf(acc[i][0]) + rbf(bhi(uu[1]) * dd), y1 = rbf(acc[i][1]) + rbf(blo(uu[1]) * dd);
    const float y2 = rbf(acc[i][2]) + rbf(bhi(uu[0]) * dd), y3 = rbf(acc[i][3]) + rbf(blo(uu[0]) * dd);
    u32x2 o = {pk2(y0, y1), pk2(y2, y3)};
    *(u32x2*)(ws + WS_Y2T + (size_t)ch * 4096 + s * 2) = o;
  }
}

__global__ __launch_bounds__(256) void ka3(const u16* __restrict__ Q, const char* __restrict__ ws, u16* __restrict__ out) {
  __shared__ __attribute__((aligned(16))) u16 tile[64 * 136];
  const int xcd = blockIdx.x & 7, idx = blockIdx.x >> 3;
  const int cg = xcd * 2 + (idx & 1), sb = idx >> 1;
  const int tid = threadIdx.x, c8 = tid & 7, r4 = tid >> 3;
  const int ch0 = cg * 64 + c8 * 8, s0 = sb * 128 + r4 * 4;
  u32x4 qr[4];
#pragma unroll
  for (int i = 0; i < 4; ++i) qr[i] = *(const u32x4*)(Q + (size_t)(s0 + i) * 1024 + ch0);
#pragma unroll
  for (int it = 0; it < 4; ++it) {
    const int e = it * 256 + tid, ch = e >> 4, c16 = e & 15;
    *(u32x4*)(tile + ch * 136 + c16 * 8) = *(const u32x4*)(ws + WS_Y2T + (size_t)(cg * 64 + ch) * 4096 + sb * 256 + c16 * 16);
  }
  __syncthreads();
  u32x2 y[8];
#pragma unroll
  for (int c = 0; c < 8; ++c) y[c] = *(const u32x2*)(tile + (c8 * 8 + c) * 136 + 4 * r4);
#pragma unroll
  for (int r = 0; r < 4; ++r) {
    u32x4 o;
#pragma unroll
    for (int cp = 0; cp < 4; ++cp) {
      const unsigned ya = y[2 * cp][r >> 1], yb = y[2 * cp + 1][r >> 1];
      const float fa = (r & 1) ? bhi(ya) : blo(ya), fb = (r & 1) ? bhi(yb) : blo(yb);
      o[cp] = pk2(blo(qr[r][cp]) * fa, bhi(qr[r][cp]) * fb);
    }
    *(u32x4*)(out + (size_t)(s0 + r) * 2048 + ch0) = o;
  }
}

extern "C" void kernel_launch(void* const* d_in, const int* in_sizes, int n_in, void* d_out, int out_size, void* d_ws, size_t ws_size, hipStream_t stream) {
  (void)hipFuncSetAttribute((const void*)k1, hipFuncAttributeMaxDynamicSharedMemorySize, K1_LDS);
  (void)hipFuncSetAttribute((const void*)kg2, hipFuncAttributeMaxDynamicSharedMemorySize, K3_LDS);
  char* ws = (char*)d_ws; u16* out = (u16*)d_out;
  ka1<<<256, 256, 0, stream>>>((const u16*)d_in[1], (const u16*)d_in[2], (const u16*)d_in[3], ws);
  k1<<<256, 512, K1_LDS, stream>>>((const u16*)d_in[6], (const u16*)d_in[7], (const u16*)d_in[8], ws);
  kconv<<<256, 256, 0, stream>>>((const u16*)d_in[4], (const u16*)d_in[5], ws);
  kred<<<32, 256, 0, stream>>>(ws);
  ka3<<<256, 256, 0, stream>>>((const u16*)d_in[0], ws, out);
  ko<<<512, 256, 0, stream>>>(ws);
  kg2<<<256, 512, K3_LDS, stream>>>((const u16*)d_in[9], ws, out);
}
```

```cpp
#include <hip/hip_runtime.h>
#include <hip/hip_bf16.h>
#include <stdint.h>

#define LAS __attribute__((address_space(3)))
#define GAS __attribute__((address_space(1)))
#define DI __device__ __forceinline__
typedef unsigned short u16;
typedef unsigned long long u64;
typedef __bf16 bf16x8 __attribute__((ext_vector_type(8)));
typedef __bf16 bf16x4 __attribute__((ext_vector_type(4)));
typedef __bf16 bf16x2 __attribute__((ext_vector_type(2)));
typedef float f32x16 __attribute__((ext_vector_type(16)));
typedef float f32x4 __attribute__((ext_vector_type(4)));
typedef float f32x2 __attribute__((ext_vector_type(2)));
typedef unsigned u32x4 __attribute__((ext_vector_type(4)));
typedef unsigned u32x2 __attribute__((ext_vector_type(2)));

#if defined(__HIP_DEVICE_COMPILE__)
#pragma clang attribute push(__attribute__((target("no-packed-fp32-ops"))), apply_to = function)
#endif
#define FBITS(x) __builtin_bit_cast(unsigned, (float)(x))
#define UFLT(x) __builtin_bit_cast(float, (unsigned)(x))
#define BALLOT(p) __builtin_amdgcn_ballot_w64(p)
#define ALL(p) (__builtin_amdgcn_ballot_w64(!(p)) == 0)
constexpr unsigned WS_EPOCH = 0;
constexpr unsigned WS_F1 = 4096;
constexpr unsigned WS_F4 = WS_F1 + 8192;
constexpr unsigned WS_F5 = WS_F4 + 8192;
constexpr unsigned WS_ZPART = 1u << 20;
constexpr unsigned WS_KVF = 2u << 20;
constexpr unsigned WS_ZF = 3u << 20;
constexpr unsigned WS_UR = 8u << 20;
constexpr unsigned WS_Y2T = 12u << 20;
constexpr unsigned WS_OF = 16u << 20;
constexpr unsigned WS_PART = 20u << 20;
constexpr unsigned WS_END = 28u << 20;

constexpr int STG = 40960;
constexpr int L_SPARE = 3 * STG;
constexpr int L_A1 = L_SPARE;
constexpr int L_H = L_SPARE;
constexpr int L_U = 2 * STG;
constexpr int L_PK = 0;
constexpr int L_V = 141312;
constexpr int L_KH0 = 115712, L_KH3 = 157696;
constexpr int L_TCH = 161792;
constexpr int L_CTL = 163712;
constexpr int LDS_TOTAL = 163840;
constexpr int C_TGT = 0, C_IMG = 8, C_D15 = 16, C_KV = 20, C_CONV = 24, C_WO3 = 36, C_KH = 40, C_P1 = 56, C_P2 = 60, C_P4 = 64, C_PF5 = 72, C_OP = 76;

#define MFMA32(a, b, c) __builtin_amdgcn_mfma_f32_32x32x16_bf16((a), (b), (c), 0, 0, 0)
#define MFMA16(a, b, c) __builtin_amdgcn_mfma_f32_16x16x32_bf16((a), (b), (c), 0, 0, 0)
#define WAITV(n) asm volatile("s_waitcnt vmcnt(" #n ")" ::: "memory")
#define WAITL(n) asm volatile("s_waitcnt lgkmcnt(" #n ")" ::: "memory")
#define FENCE() asm volatile("" ::: "memory")
#define BAR() do { FENCE(); __builtin_amdgcn_s_barrier(); FENCE(); } while (0)
#define SCHED() __builtin_amdgcn_sched_barrier(0)
#define RLX_AGENT __ATOMIC_RELAXED, __HIP_MEMORY_SCOPE_AGENT
#define RLX_WG __ATOMIC_RELAXED, __HIP_MEMORY_SCOPE_WORKGROUP
constexpr unsigned SPIN_CAP = 1u << 20;

constexpr int PSLEEP = 4;
constexpr int BURST = 3;

DI void dma16(unsigned voff, const void* sbase, unsigned lds) {
  unsigned keep;
  asm volatile("s_mov_b32 %0, m0\n\ts_mov_b32 m0, %3\n\ts_nop 0\n\tglobal_load_lds_dwordx4 %1, %2\n\ts_mov_b32 m0, %0" : "=&s"(keep) : "v"(voff), "s"(sbase), "s"(lds) : "memory");
}
DI void dma4(const void* g, unsigned lds) {
  unsigned keep;
  asm volatile("s_mov_b32 %0, m0\n\ts_mov_b32 m0, %2\n\ts_nop 0\n\tglobal_load_lds_dword %1, off\n\ts_mov_b32 m0, %0" : "=&s"(keep) : "v"(g), "s"(lds) : "memory");
}
DI bf16x8 trfrag(unsigned a0, unsigned d) {
  bf16x4 lo = __builtin_amdgcn_ds_read_tr16_b64_v4bf16((LAS bf16x4*)(a0));
  bf16x4 hi = __builtin_amdgcn_ds_read_tr16_b64_v4bf16((LAS bf16x4*)(a0 + d));
  return __builtin_shufflevector(lo, hi, 0, 1, 2, 3, 4, 5, 6, 7);
}
DI bf16x8 pack8(const f32x16& x, int s) {
  bf16x8 r;
#pragma unroll
  for (int j = 0; j < 8; ++j) r[j] = (__bf16)x[8 * s + j];
  return r;
}
DI float rbf(float x) { return (float)(__bf16)x; }
DI float blo(unsigned x) { return UFLT(x << 16); }
DI float bhi(unsigned x) { return UFLT(x & 0xffff0000u); }
DI f32x2 unpk(unsigned x) { return (f32x2){blo(x), bhi(x)}; }
DI unsigned pk2(float a, float b) { bf16x2 v = {(__bf16)a, (__bf16)b}; return __builtin_bit_cast(unsigned, v); }
DI float xswap(float x) {
  auto r = __builtin_amdgcn_permlane32_swap(FBITS(x), FBITS(x), false, false);
  return UFLT((__builtin_amdgcn_workitem_id_x() & 32) ? r[0] : r[1]);
}
DI unsigned shx(unsigned x, int m) { return (unsigned)__builtin_amdgcn_ds_bpermute((int)(((__builtin_amdgcn_workitem_id_x() & 63) ^ m) << 2), (int)x); }
DI float shxf(float x, int m) { return UFLT(shx(FBITS(x), m)); }
template <class R> DI bool flags_up(R wsr, unsigned off, u64 tgt) {
  FENCE();
  const u32x2 v = __builtin_amdgcn_raw_buffer_load_b64(wsr, off, 0, 16);
  return ALL(v[0] == (unsigned)tgt && v[1] == (unsigned)(tgt >> 32));
}
template <class R> DI void wait_flags(R wsr, unsigned off, u64 tgt, int lane) {
#pragma unroll 1
  for (unsigned spin = 0; spin < SPIN_CAP; ++spin) {
    if (flags_up(wsr, off + lane * 8, tgt)) break;
    __builtin_amdgcn_s_sleep(PSLEEP);
  }
  FENCE();
}
template <class R> DI void set_flag(R wsr, unsigned off, u64 tgt, int lane) {
  WAITV(0);
  if (lane == 0) __builtin_amdgcn_raw_buffer_store_b64((u32x2){(unsigned)tgt, (unsigned)(tgt >> 32)}, wsr, off, 0, 16);
}
DI void lds_signal(unsigned a, int lane) { WAITL(0); if (lane == 0) __hip_atomic_fetch_add((LAS unsigned*)a, 1u, RLX_WG); }
DI void lds_wait(unsigned a, unsigned n) {
#pragma unroll 1
  for (unsigned spin = 0; spin < SPIN_CAP; ++spin) {
    if (__builtin_amdgcn_readfirstlane(__hip_atomic_load((LAS unsigned*)a, RLX_WG)) >= n) break;
    __builtin_amdgcn_s_sleep(1);
  }
  FENCE();
}
template <class R> DI void group_wait_flags(R wsr, unsigned off, u64 tgt, int lane, bool leader, unsigned ldsw) {
  if (leader) {
    wait_flags(wsr, off, tgt, lane);
    if (lane == 0) __hip_atomic_store((LAS unsigned*)ldsw, 1u, RLX_WG);
  } else lds_wait(ldsw, 1);
}
DI u32x2 enc20(float a, float b, unsigned tag24) {
  const unsigned x = (FBITS(a) + 0x800u) >> 12, y = (FBITS(b) + 0x800u) >> 12;
  return (u32x2){x | (y << 20), (y >> 12) | (tag24 << 8)};
}
DI float dec20a(unsigned lo) { return UFLT(lo << 12); }
DI float dec20b(unsigned lo, unsigned hi) { return UFLT(__builtin_amdgcn_alignbit(hi, lo, 20) << 12); }
DI void featmap(f32x16 p, bf16x8& f0, bf16x8& f1, bf16x8& f2, bf16x8& f3) {
  constexpr float L2E = 1.4426950408889634f;
#pragma unroll
  for (int i = 0; i < 16; i += 2) { const unsigned u = pk2(p[i], p[i + 1]); p[i] = blo(u); p[i + 1] = bhi(u); }
  float mx = __builtin_fmaxf(p[0], p[1]), mn = __builtin_fminf(p[0], p[1]);
#pragma unroll
  for (int i = 2; i < 16; i += 2) { mx = __builtin_fmaxf(__builtin_fmaxf(mx, p[i]), p[i + 1]); mn = __builtin_fminf(__builtin_fminf(mn, p[i]), p[i + 1]); }
  mx = __builtin_fmaxf(mx, xswap(mx)); mn = __builtin_fminf(mn, xswap(mn));
  const f32x2 a = {-mx * L2E, -mx * L2E}, b = {mn * L2E, mn * L2E}, c1 = {L2E, L2E}, c2 = {-L2E, -L2E};
  f32x2 e1[8], e2[8];
#pragma unroll
  for (int i = 0; i < 8; ++i) {
    const f32x2 pp = {p[2 * i], p[2 * i + 1]};
    const f32x2 x1 = __builtin_elementwise_fma(pp, c1, a), x2 = __builtin_elementwise_fma(pp, c2, b);
    e1[i] = (f32x2){__builtin_amdgcn_exp2f(x1[0]), __builtin_amdgcn_exp2f(x1[1])};
    e2[i] = (f32x2){__builtin_amdgcn_exp2f(x2[0]), __builtin_amdgcn_exp2f(x2[1])};
  }
  f32x2 s1 = e1[0], s2 = e2[0];
#pragma unroll
  for (int i = 1; i < 8; ++i) { s1 += e1[i]; s2 += e2[i]; }
  float t1 = s1[0] + s1[1], t2 = s2[0] + s2[1];
  t1 = __builtin_amdgcn_rcpf(t1 + xswap(t1)); t2 = __builtin_amdgcn_rcpf(t2 + xswap(t2));
  u32x4 r0, r1, r2, r3;
#pragma unroll
  for (int i = 0; i < 4; ++i) {
    const f32x2 a0 = e1[i] * t1, a1 = e1[4 + i] * t1, b0 = e2[i] * t2, b1 = e2[4 + i] * t2;
    r0[i] = pk2(a0[0], a0[1]); r1[i] = pk2(a1[0], a1[1]); r2[i] = pk2(b0[0], b0[1]); r3[i] = pk2(b1[0], b1[1]);
  }
  f0 = __builtin_bit_cast(bf16x8, r0); f1 = __builtin_bit_cast(bf16x8, r1); f2 = __builtin_bit_cast(bf16x8, r2); f3 = __builtin_bit_cast(bf16x8, r3);
}
DI void pk_write(unsigned pk_img, int n, int h, bf16x8 f0, bf16x8 f1, bf16x8 f2, bf16x8 f3) {
  const int g = (n & 1) | (((n >> 2) & 3) << 1) | (((n >> 1) & 1) << 3);
  const unsigned rowa = pk_img + n * 128;
  auto wr = [&](int u, bf16x4 v) { *(LAS bf16x4*)(rowa + ((u ^ g) << 3)) = v; };
  wr(0 + h, __builtin_shufflevector(f0, f0, 0, 1, 2, 3)); wr(2 + h, __builtin_shufflevector(f0, f0, 4, 5, 6, 7));
  wr(4 + h, __builtin_shufflevector(f1, f1, 0, 1, 2, 3)); wr(6 + h, __builtin_shufflevector(f1, f1, 4, 5, 6, 7));
  wr(8 + h, __builtin_shufflevector(f2, f2, 0, 1, 2, 3)); wr(10 + h, __builtin_shufflevector(f2, f2, 4, 5, 6, 7));
  wr(12 + h, __builtin_shufflevector(f3, f3, 0, 1, 2, 3)); wr(14 + h, __builtin_shufflevector(f3, f3, 4, 5, 6, 7));
}

struct Args { const u16 *q, *k, *v, *sk, *h, *dv, *x, *wqkv, *wphi, *wo; u16* out; char* ws; };

__global__ __launch_bounds__(512, 2) void fused(Args A) {
  extern __shared__ __attribute__((aligned(16))) char sm[];
  char* const ws = A.ws;
  const int wave = __builtin_amdgcn_readfirstlane(__builtin_amdgcn_workitem_id_x() >> 6);
  const int lane = __builtin_amdgcn_workitem_id_x() & 63;
  const int xcd = __builtin_amdgcn_workgroup_id_x() & 7, idx = __builtin_amdgcn_workgroup_id_x() >> 3;
  const int rb = (xcd & 1) * 8 + (idx & 7), hp = (xcd >> 1) * 4 + (idx >> 3);
  const int cg = xcd * 2 + (idx & 1), sb = idx >> 1;
  const int rb2 = (xcd >> 1) * 4 + (idx >> 3), cb = (xcd & 1) * 8 + (idx & 7);
  const unsigned smb = (unsigned)(size_t)sm;
  const auto wsr = __builtin_amdgcn_make_buffer_rsrc(ws, 0, WS_END, 0x00020000);
  const auto outr = __builtin_amdgcn_make_buffer_rsrc(A.out, 0, 8u << 20, 0x00020000);
  const int w4 = wave & 3;
  const int mi = w4 & 1, hi = w4 >> 1, head = hp * 2 + hi;
  const int h = lane >> 5, l31 = lane & 31, blk = (lane >> 4) & 1, q4 = (lane & 15) >> 2, p4 = lane & 3;
  const int lr = lane >> 3, lc = lane & 7;
  const int ch0 = cg * 64 + lc * 8, S0 = sb * 128 + w4 * 32, s0 = S0 + lr * 4;
  const unsigned pk_img = smb + L_PK + hi * 16384, v_img = smb + L_V + hi * 8192;
  const unsigned kh_buf = smb + (w4 < 3 ? L_KH0 + w4 * 2048 : L_KH3) + lane * 16;
  u64 tgt;
  const unsigned ovo = lr * 2048 + ((lc ^ (((lr >> 1) & 1) << 2)) << 4);
  auto wo_piece = [&](int pc) { dma16(ovo, (const char*)A.wo + cb * 128 + (size_t)pc * 16384, smb + pc * 1024); };
  auto load_wf = [&](bf16x8 (&wf)[2]) {
#pragma unroll
    for (int s = 0; s < 2; ++s)
#pragma unroll
      for (int j = 0; j < 8; ++j) { const int d = 16 * s + 8 * (j >> 2) + 4 * h + (j & 3); wf[s][j] = __builtin_bit_cast(__bf16, A.wphi[head * 1024 + d * 32 + l31]); }
  };
  auto img_addr = [&](int t, unsigned& pa, unsigned& va) {
    const int n = 8 * h + 4 * t + q4;
    const int g = (n & 1) | (((n >> 2) & 3) << 1) | (((n >> 1) & 1) << 3);
    pa = pk_img + n * 128 + (((8 * mi + 4 * blk + p4) ^ g) << 3);
    va = v_img + n * 64 + (((4 * blk + p4) ^ ((n >> 1) & 7)) << 3);
  };

  if (wave >= 4) {
    const int lw = w4;
    unsigned xvo[4];
#pragma unroll
    for (int i = 0; i < 4; ++i) { const int r = 32 * lw + 8 * i + lr; xvo[i] = r * 2048 + ((lc ^ ((r >> 1) & 7)) << 4); }
    const unsigned wvo = lr * 6144 + ((lc ^ (((lr >> 1) & 1) << 2)) << 4);
    const char* const xsb = (const char*)A.x + (size_t)rb * 262144;
    const char* const wsb = (const char*)A.wqkv + hp * 128;
    auto issue = [&](int t, int slot) {
      const unsigned sbp = smb + slot * STG;
#pragma unroll
      for (int i = 0; i < 4; ++i) dma16(xvo[i], xsb + t * 128, sbp + (32 * lw + 8 * i) * 128);
#pragma unroll
      for (int j = 0; j < 6; ++j) { const int id = lw * 6 + j; dma16(wvo, wsb + (size_t)(t * 8 + (id & 7)) * 49152 + (id >> 3) * 2048, sbp + 16384 + id * 1024); }
    };
    const int j4 = idx >> 3;
    const char* tpa; unsigned tps;
    { const int e = lw * 56 + (lane < 56 ? lane : 55);
      if (e < 128) { tpa = (const char*)A.x + (size_t)(rb * 128 + e) * 2048; tps = 128; }
      else { const int l = e - 128, kr = 8 * (idx & 7) + l / 12, r = l % 12; tpa = (const char*)A.wqkv + ((size_t)kr * 3072 + (r >> 2) * 1024 + ((xcd >> 1) * 4 + (r & 3)) * 64) * 2; tps = 64 * 6144; } }
    auto touch = [&](const char* p) { dma4(p, smb + L_TCH + lw * 256); };
    const char* tad[4];
#pragma unroll
    for (int i = 0; i < 4; ++i) tad[i] = tpa + (size_t)(j4 + 4 * i) * tps;
    const char* two = (const char*)A.wo + (size_t)(j4 * 256 + lw * 64 + lane) * 2048 + cb * 128;
    asm volatile("" : "+v"(tad[1]), "+v"(tad[2]), "+v"(tad[3]), "+v"(two));
    __builtin_amdgcn_s_sleep(3);
    touch(tad[0]);
    if (j4 + 4 <= BURST + 1) touch(tad[1]);
    issue(0, 0); issue(1, 1);
    WAITV(10); BAR();
    int prev = 0, slot = 2;
#pragma unroll 1
    for (int t = 0; t < 13; ++t) {
      issue(t + 2, slot); slot = slot == 2 ? 0 : slot + 1;
      int now = 0;
      const int d = t + 2 + BURST - j4;
      if (d == 4) { touch(tad[1]); now = 1; }
      if (d == 8) { touch(tad[2]); now = 1; }
      if (d == 12) { touch(tad[3]); now = 1; }
      const int n = now + prev;
      if (n == 0) WAITV(10); else if (n == 1) WAITV(11); else WAITV(12);
      prev = now;
      BAR();
    }
    issue(15, 0);
    FENCE();
    const int ch = xcd * 128 + idx * 4 + lw;
    u32x4 hv[4], uv[4], qr[4]; unsigned un[4];
    bf16x8 wf[2];
    load_wf(wf);
    tgt = *(const LAS u64*)(smb + L_CTL + C_TGT);
    const u32x2 f1v = __builtin_amdgcn_raw_buffer_load_b64(wsr, WS_F1 + (xcd * 2 + (idx >> 4)) * 512 + lane * 8, 0, 16);
#pragma unroll
    for (int i = 0; i < 4; ++i) hv[i] = __builtin_nontemporal_load((const u32x4*)((const char*)A.h + (size_t)ch * 4096 + i * 1024 + lane * 16));
#pragma unroll
    for (int i = 0; i < 4; ++i) qr[i] = __builtin_nontemporal_load((const u32x4*)(A.q + (size_t)(s0 + i) * 1024 + ch0));
    const unsigned ddr = A.dv[ch];
    touch(two);
    WAITV(36); BAR();
    WAITV(26); BAR();
    auto load_u = [&]() {
#pragma unroll
      for (int i = 0; i < 4; ++i) {
        uv[i] = __builtin_amdgcn_raw_buffer_load_b128(wsr, WS_UR + ch * 4096 + i * 1024 + lane * 16, 0, 16);
        un[i] = __builtin_amdgcn_raw_buffer_load_b32(wsr, WS_UR + ch * 4096 + ((i * 1024 + lane * 16 + 16) & 4095), 0, 16);
      }
    };
    const bool uok = ALL(f1v[0] == (unsigned)tgt && f1v[1] == (unsigned)(tgt >> 32));
    if (uok) load_u();
    const unsigned hb = smb + L_H + lw * 4608, ub = smb + L_U + lw * 8448;
    {
      unsigned pa[2], va[2];
      img_addr(0, pa[0], va[0]); img_addr(1, pa[1], va[1]);
      lds_wait(smb + L_CTL + C_KH + w4 * 4, 1);
      const bf16x8 b0 = *(const LAS bf16x8*)(kh_buf), b1 = *(const LAS bf16x8*)(kh_buf + 1024);
      f32x16 p = (f32x16){};
      p = MFMA32(wf[0], b0, p); p = MFMA32(wf[1], b1, p);
      bf16x8 f0, f1, f2, f3;
      featmap(p, f0, f1, f2, f3);
      lds_wait(smb + L_CTL + C_D15, 4);
      pk_write(pk_img, 64 * mi + 32 + l31, h, f0, f1, f2, f3);
      lds_signal(smb + L_CTL + C_IMG + hi * 4, lane);
      lds_wait(smb + L_CTL + C_IMG + hi * 4, 4);
      f32x16 dz = (f32x16){};
      bf16x8 ones;
#pragma unroll
      for (int j = 0; j < 8; ++j) ones[j] = (__bf16)1.0f;
#pragma unroll
      for (int ks = 0; ks < 8; ++ks) {
        const bf16x4 a0 = __builtin_amdgcn_ds_read_tr16_b64_v4bf16((LAS bf16x4*)(pa[0] + ks * 2048)), a1 = __builtin_amdgcn_ds_read_tr16_b64_v4bf16((LAS bf16x4*)(pa[1] + ks * 2048));
        dz = MFMA32(__builtin_shufflevector(a0, a1, 0, 1, 2, 3, 4, 5, 6, 7), ones, dz);
      }
      const unsigned tlo = (unsigned)tgt, thi = (unsigned)(tgt >> 32);
      if (l31 == 0) {
#pragma unroll
        for (int a = 0; a < 4; ++a)
#pragma unroll
          for (int e = 0; e < 2; ++e)
            __builtin_amdgcn_raw_buffer_store_b128((u32x4){FBITS(dz[4 * a + 2 * e]), tlo, FBITS(dz[4 * a + 2 * e + 1]), thi}, wsr, WS_ZPART + ((((hp * 16 + hi * 8 + mi * 4 + a) * 16 + rb) * 2 + h) * 2 + e) * 16, 0, 16);
      }
      lds_signal(smb + L_CTL + C_KV, lane);
    }
    if (!uok) { wait_flags(wsr, WS_F1 + (xcd * 2 + (idx >> 4)) * 512, tgt, lane); load_u(); }
    if (lane < 32) *(LAS u32x4*)(hb + lane * 16) = (u32x4){0, 0, 0, 0};
#pragma unroll
    for (int i = 0; i < 4; ++i) *(LAS u32x4*)(hb + 512 + i * 1024 + lane * 16) = hv[i];
    if (lane == 63) un[3] = 0;
    if (lane < 2) { *(LAS u32x4*)(ub + 4096 + lane * 16) = (u32x4){0, 0, 0, 0}; *(LAS u32x4*)(ub + 4160 + 4096 + lane * 16) = (u32x4){0, 0, 0, 0}; }
#pragma unroll
    for (int i = 0; i < 4; ++i) {
      *(LAS u32x4*)(ub + i * 1024 + lane * 16) = uv[i];
      const u32x4 sh = {__builtin_amdgcn_alignbit(uv[i][1], uv[i][0], 16), __builtin_amdgcn_alignbit(uv[i][2], uv[i][1], 16), __builtin_amdgcn_alignbit(uv[i][3], uv[i][2], 16), __builtin_amdgcn_alignbit(un[i], uv[i][3], 16)};
      *(LAS u32x4*)(ub + 4160 + i * 1024 + lane * 16) = sh;
    }
    const float dd = blo(ddr);
    asm volatile("" :: "v"(qr[0]), "v"(qr[1]), "v"(qr[2]), "v"(qr[3]), "v"(dd));
    WAITL(0);
    const int m = lane & 15, g = lane >> 4;
    const int st0 = 2055 - 8 * g - m;
    const unsigned aa = ((m & 1) ? ub + st0 * 2 : ub + 4160 + (st0 - 1) * 2) - 3584;
    const unsigned ba = hb + 512 + (16 * m + 8 - 8 * g) * 2;
    f32x4 cacc[8];
#pragma unroll
    for (int i = 0; i < 8; ++i) cacc[i] = (f32x4){};
    bf16x8 Af[2][8], Bf[2][8];
    auto cld1 = [&](int b, unsigned ar, unsigned br, int p) {
      const LAS unsigned* pa = (const LAS unsigned*)(ar + 512 * (7 - p));
      const u32x4 t = {pa[0], pa[1], pa[2], pa[3]};
      Af[b][p] = __builtin_bit_cast(bf16x8, t);
      Bf[b][p] = *(const LAS bf16x8*)(br + 512 * p);
    };
    auto cstep = [&](int b, int rn, bool more) {
      const unsigned ar = aa - 64 * rn, br = ba - 64 * rn;
#pragma unroll
      for (int i = 0; i < 8; ++i) {
        if (more) cld1(b ^ 1, ar, br, i);
#pragma unroll
        for (int p = 0; p <= i; ++p) cacc[i] = MFMA16(Af[b][p], Bf[b][i - p], cacc[i]);
        SCHED();
      }
    };
#pragma unroll
    for (int p = 0; p < 8; ++p) cld1(0, aa, ba, p);
    for (int r = 0; r < 6; r += 2) { cstep(0, r + 1, true); cstep(1, r + 2, true); }
    cstep(0, 7, true); cstep(1, 0, false);
#pragma unroll
    for (int i = 0; i < 8; ++i) {
      const int s = 256 * i + 16 * m + 4 * g;
      const u32x2 uu = *(const LAS u32x2*)(ub + (2044 - s) * 2);
      const float y0 = rbf(cacc[i][0]) + rbf(bhi(uu[1]) * dd), y1 = rbf(cacc[i][1]) + rbf(blo(uu[1]) * dd);
      const float y2 = rbf(cacc[i][2]) + rbf(bhi(uu[0]) * dd), y3 = rbf(cacc[i][3]) + rbf(blo(uu[0]) * dd);
      __builtin_amdgcn_raw_buffer_store_b64((u32x2){pk2(y0, y1), pk2(y2, y3)}, wsr, WS_Y2T + ch * 4096 + s * 2, 0, 16);
    }
    lds_signal(smb + L_CTL + C_CONV, lane);
    set_flag(wsr, WS_F5 + ((xcd * 2 + (idx >> 4)) * 64 + (idx & 15) * 4 + lw) * 8, tgt, lane);
    if (__builtin_amdgcn_workgroup_id_x() == 0 && wave == 5) {
#pragma unroll 1
      for (unsigned spin = 0; spin < SPIN_CAP; ++spin) {
        bool ok = true;
        u32x2 v[16];
        FENCE();
#pragma unroll
        for (int i = 0; i < 16; ++i) v[i] = __builtin_amdgcn_raw_buffer_load_b64(wsr, WS_F1 + i * 512 + lane * 8, 0, 16);
#pragma unroll
        for (int i = 0; i < 16; ++i) ok = ok && v[i][0] == (unsigned)tgt && v[i][1] == (unsigned)(tgt >> 32);
        if (ALL(ok)) break;
        __builtin_amdgcn_s_sleep(16);
      }
      if (lane == 0) __builtin_amdgcn_raw_buffer_store_b64((u32x2){(unsigned)tgt, (unsigned)(tgt >> 32)}, wsr, WS_EPOCH, 0, 16);
    }
    lds_wait(smb + L_CTL + C_CONV, 4);
    lds_wait(smb + L_CTL + C_OP, 4);
    for (int i = 20; i < 32; ++i) wo_piece(i * 4 + lw);
    WAITV(0);
    lds_signal(smb + L_CTL + C_WO3, lane);
    {
      u32x4 yv[4];
      group_wait_flags(wsr, WS_F5 + cg * 512, tgt, lane, lw == 0, smb + L_CTL + C_PF5);
#pragma unroll
      for (int it = 0; it < 4; ++it) yv[it] = __builtin_amdgcn_raw_buffer_load_b128(wsr, WS_Y2T + (cg * 64 + it * 16 + (lane >> 2)) * 4096 + S0 * 2 + (lane & 3) * 16, 0, 16);
      const unsigned tb = smb + L_V + w4 * 4096;
#pragma unroll
      for (int it = 0; it < 4; ++it) { const int c = it * 16 + (lane >> 2); *(LAS u32x4*)(tb + c * 64 + (((lane & 3) ^ ((c >> 3) & 3)) << 4)) = yv[it]; }
      WAITL(0);
      u32x2 y[8];
#pragma unroll
      for (int c = 0; c < 8; ++c) y[c] = *(const LAS u32x2*)(tb + (lc * 8 + c) * 64 + ((((lr >> 1) ^ (lc & 3)) << 4) | ((lr & 1) << 3)));
#pragma unroll
      for (int r = 0; r < 4; ++r) {
        u32x4 o;
#pragma unroll
        for (int cp = 0; cp < 4; ++cp) {
          const unsigned ya = y[2 * cp][r >> 1], yb = y[2 * cp + 1][r >> 1];
          const float fa = (r & 1) ? bhi(ya) : blo(ya), fb = (r & 1) ? bhi(yb) : blo(yb);
          o[cp] = pk2(blo(qr[r][cp]) * fa, bhi(qr[r][cp]) * fb);
        }
        __builtin_amdgcn_raw_buffer_store_b128(o, outr, ((s0 + r) * 2048 + ch0) * 2, 0, 16);
      }
    }
    return;
  }
  {
  bf16x8 wf[2];
  {
    u32x4 kr[7], vr[4], wv[4];
#pragma unroll
    for (int i = 0; i < 7; ++i) { const int s = s0 - 3 + i; kr[i] = __builtin_nontemporal_load((const u32x4*)(A.k + (size_t)(s < 0 ? 0 : s) * 1024 + ch0)); }
#pragma unroll
    for (int i = 0; i < 4; ++i) vr[i] = __builtin_nontemporal_load((const u32x4*)(A.v + (size_t)(s0 + i) * 1024 + ch0));
#pragma unroll
    for (int i = 0; i < 4; ++i) wv[i] = *(const u32x4*)(A.sk + (size_t)ch0 * 4 + i * 8);
    const u64 ep = __hip_atomic_load((const GAS u64*)(ws + WS_EPOCH), RLX_AGENT);
    load_wf(wf);
    tgt = (ep ^ 0x5851F42D4C957F2DULL) * 0x9E3779B97F4A7C15ULL + 0x2545F4914F6CDD1DULL;
    if (wave == 0 && lane < 32) *(LAS unsigned*)(smb + L_CTL + lane * 4) = lane == 0 ? (unsigned)tgt : lane == 1 ? (unsigned)(tgt >> 32) : 0u;
#pragma unroll
    for (int i = 0; i < 3; ++i) if (s0 - 3 + i < 0) kr[i] = (u32x4){0, 0, 0, 0};
    const unsigned tb = smb + L_A1 + w4 * 4096;
    const int un8 = 7 - lr;
    const unsigned wadr = tb + lc * 512 + ((((un8 >> 1) ^ (lc & 3)) << 4) | ((un8 & 1) << 3));
#pragma unroll
    for (int cp = 0; cp < 4; ++cp) {
      const f32x2 w0 = {blo(wv[cp][0]), blo(wv[cp][2])}, w1 = {bhi(wv[cp][0]), bhi(wv[cp][2])};
      const f32x2 w2 = {blo(wv[cp][1]), blo(wv[cp][3])}, w3 = {bhi(wv[cp][1]), bhi(wv[cp][3])};
      f32x2 kk[7], u[4];
#pragma unroll
      for (int i = 0; i < 7; ++i) kk[i] = unpk(kr[i][cp]);
#pragma unroll
      for (int r = 0; r < 4; ++r) {
        f32x2 a = kk[r] * w3;
        a = __builtin_elementwise_fma(kk[r + 1], w2, a);
        a = __builtin_elementwise_fma(kk[r + 2], w1, a);
        a = __builtin_elementwise_fma(kk[r + 3], w0, a);
        u[r] = a * unpk(vr[r][cp]);
      }
      *(LAS u32x2*)(wadr + (2 * cp) * 64) = (u32x2){pk2(u[3][0], u[2][0]), pk2(u[1][0], u[0][0])};
      *(LAS u32x2*)(wadr + (2 * cp + 1) * 64) = (u32x2){pk2(u[3][1], u[2][1]), pk2(u[1][1], u[0][1])};
    }
    WAITL(0);
#pragma unroll
    for (int it = 0; it < 4; ++it) {
      const int c = it * 16 + (lane >> 2), c4 = lane & 3;
      const u32x4 v = *(const LAS u32x4*)(tb + c * 64 + ((c4 ^ ((c >> 3) & 3)) << 4));
      __builtin_amdgcn_raw_buffer_store_b128(v, wsr, WS_UR + (cg * 64 + c) * 4096 + (2016 - S0) * 2 + c4 * 16, 0, 16);
    }
  }
  unsigned xa[4];
  { const int r = 64 * mi + l31;
#pragma unroll
    for (int s = 0; s < 4; ++s) xa[s] = smb + r * 128 + (((2 * s + h) ^ ((r >> 1) & 7)) << 4); }
  const unsigned wa = smb + 16384 + (8 * h + q4) * 128 + (((4 * hi + 2 * blk + (p4 >> 1)) ^ ((q4 >> 1) << 2)) << 4) + 8 * (p4 & 1);
  f32x16 acc[3][2];
#pragma unroll
  for (int c = 0; c < 3; ++c) { acc[c][0] = (f32x16){}; acc[c][1] = (f32x16){}; }
  bf16x8 fx[2][2], fw[2][3];
  auto ld = [&](int b, unsigned so, int s) {
#pragma unroll
    for (int c = 0; c < 3; ++c) fw[b][c] = trfrag(wa + so + c * 8192 + s * 2048, 512);
#pragma unroll
    for (int tt = 0; tt < 2; ++tt) fx[b][tt] = *(const LAS bf16x8*)(xa[s] + so + tt * 4096);
  };
  auto mm = [&](int b) {
#pragma unroll
    for (int c = 0; c < 3; ++c)
#pragma unroll
      for (int tt = 0; tt < 2; ++tt) acc[c][tt] = MFMA32(fw[b][c], fx[b][tt], acc[c][tt]);
  };
  auto mmld = [&](int b, unsigned so, int s) {
    const int n = b ^ 1;
    acc[0][0] = MFMA32(fw[b][0], fx[b][0], acc[0][0]); SCHED();
    fw[n][0] = trfrag(wa + so + s * 2048, 512); SCHED();
    acc[0][1] = MFMA32(fw[b][0], fx[b][1], acc[0][1]); SCHED();
    fx[n][0] = *(const LAS bf16x8*)(xa[s] + so); fx[n][1] = *(const LAS bf16x8*)(xa[s] + so + 4096); SCHED();
    acc[1][0] = MFMA32(fw[b][1], fx[b][0], acc[1][0]); SCHED();
    fw[n][1] = trfrag(wa + so + 8192 + s * 2048, 512); SCHED();
    acc[1][1] = MFMA32(fw[b][1], fx[b][1], acc[1][1]); SCHED();
    fw[n][2] = trfrag(wa + so + 16384 + s * 2048, 512); SCHED();
    acc[2][0] = MFMA32(fw[b][2], fx[b][0], acc[2][0]); SCHED();
    acc[2][1] = MFMA32(fw[b][2], fx[b][1], acc[2][1]); SCHED();
  };
  auto stage = [&](unsigned so, unsigned sn) {
    mmld(0, so, 1); mmld(1, so, 2); mmld(0, so, 3);
    WAITL(0); BAR();
    mmld(1, sn, 0);
  };
  WAITL(0); BAR();
  ld(0, 0, 0);
  stage(0, STG); stage(STG, 2 * STG); stage(2 * STG, 0);
  set_flag(wsr, WS_F1 + (cg * 64 + sb * 4 + w4) * 8, tgt, lane);
  for (int t = 0; t < 4; ++t) { stage(0, STG); stage(STG, 2 * STG); stage(2 * STG, 0); }
  mmld(0, 0, 1); mmld(1, 0, 2); mmld(0, 0, 3);
  lds_signal(smb + L_CTL + C_D15, lane);
  mm(1); SCHED();
  *(LAS bf16x8*)(kh_buf) = pack8(acc[1][1], 0);
  *(LAS bf16x8*)(kh_buf + 1024) = pack8(acc[1][1], 1);
  lds_signal(smb + L_CTL + C_KH + w4 * 4, lane);
  const unsigned tlo = (unsigned)tgt, thi = (unsigned)(tgt >> 32), ta = tlo & 0xffffffu, tb24 = thi & 0xffffffu;
  {
    f32x16 p = (f32x16){};
    p = MFMA32(wf[0], pack8(acc[1][0], 0), p);
    p = MFMA32(wf[1], pack8(acc[1][0], 1), p);
#pragma unroll
    for (int tt = 0; tt < 2; ++tt) {
      const int n = 64 * mi + 32 * tt + l31;
      const int g2 = (n >> 1) & 7;
      const unsigned rowv = v_img + n * 64;
      const bf16x8 v0 = pack8(acc[2][tt], 0), v1 = pack8(acc[2][tt], 1);
      *(LAS bf16x4*)(rowv + (((0 + h) ^ g2) << 3)) = __builtin_shufflevector(v0, v0, 0, 1, 2, 3);
      *(LAS bf16x4*)(rowv + (((2 + h) ^ g2) << 3)) = __builtin_shufflevector(v0, v0, 4, 5, 6, 7);
      *(LAS bf16x4*)(rowv + (((4 + h) ^ g2) << 3)) = __builtin_shufflevector(v1, v1, 0, 1, 2, 3);
      *(LAS bf16x4*)(rowv + (((6 + h) ^ g2) << 3)) = __builtin_shufflevector(v1, v1, 4, 5, 6, 7);
    }
    bf16x8 f0, f1, f2, f3;
    featmap(p, f0, f1, f2, f3);
    unsigned pa[2], va[2];
    img_addr(0, pa[0], va[0]); img_addr(1, pa[1], va[1]);
    lds_wait(smb + L_CTL + C_D15, 4);
    pk_write(pk_img, 64 * mi + l31, h, f0, f1, f2, f3);
    lds_signal(smb + L_CTL + C_IMG + hi * 4, lane);
    lds_wait(smb + L_CTL + C_IMG + hi * 4, 4);
    f32x16 dkv = (f32x16){};
#pragma unroll
    for (int ks = 0; ks < 8; ++ks) {
      const bf16x4 a0 = __builtin_amdgcn_ds_read_tr16_b64_v4bf16((LAS bf16x4*)(pa[0] + ks * 2048)), a1 = __builtin_amdgcn_ds_read_tr16_b64_v4bf16((LAS bf16x4*)(pa[1] + ks * 2048));
      const bf16x4 b0 = __builtin_amdgcn_ds_read_tr16_b64_v4bf16((LAS bf16x4*)(va[0] + ks * 1024)), b1 = __builtin_amdgcn_ds_read_tr16_b64_v4bf16((LAS bf16x4*)(va[1] + ks * 1024));
      dkv = MFMA32(__builtin_shufflevector(a0, a1, 0, 1, 2, 3, 4, 5, 6, 7), __builtin_shufflevector(b0, b1, 0, 1, 2, 3, 4, 5, 6, 7), dkv);
    }
#pragma unroll
    for (int a = 0; a < 4; ++a) {
      const u32x2 e0 = enc20(dkv[4 * a], dkv[4 * a + 1], ta), e1 = enc20(dkv[4 * a + 2], dkv[4 * a + 3], tb24);
      __builtin_amdgcn_raw_buffer_store_b128((u32x4){e0[0], e0[1], e1[0], e1[1]}, wsr, WS_PART + ((hp * 16 + hi * 8 + mi * 4 + a) * 16 + rb) * 1024 + lane * 16, 0, 16);
    }
    lds_signal(smb + L_CTL + C_KV, lane);
  }
  lds_wait(smb + L_CTL + C_KV, 8);
  for (int i = 0; i < 20; ++i) wo_piece(i * 4 + w4);
  bf16x8 phq[2][4];
#pragma unroll
  for (int tt = 0; tt < 2; ++tt) {
    f32x16 p = (f32x16){};
    p = MFMA32(wf[0], pack8(acc[0][tt], 0), p);
    p = MFMA32(wf[1], pack8(acc[0][tt], 1), p);
    featmap(p, phq[tt][0], phq[tt][1], phq[tt][2], phq[tt][3]);
  }
  {
    const int pg = lane >> 4, l15 = lane & 15;
    const unsigned base = WS_PART + ((hp * 16 + rb) * 16 + pg * 4) * 1024 + (w4 * 16 + l15) * 16;
    f32x4 s; f32x2 z;
    if (w4 == 0) {
      const int pi = lane & 15;
      const unsigned po = (lane & 16) ? WS_ZPART + (hp * 16 + rb) * 1024 + pi * 64 + 48 : WS_PART + ((hp * 16 + rb) * 16 + pi) * 1024 + 1008;
#pragma unroll 1
      for (unsigned sp = 0; sp < SPIN_CAP; ++sp) {
        FENCE();
        const u32x4 v = __builtin_amdgcn_raw_buffer_load_b128(wsr, po, 0, 16);
        if (ALL((lane & 16) ? (v[1] == tlo && v[3] == thi) : ((v[1] >> 8) == ta && (v[3] >> 8) == tb24))) break;
        __builtin_amdgcn_s_sleep(PSLEEP);
      }
      if (lane == 0) __hip_atomic_store((LAS unsigned*)(smb + L_CTL + C_P1), 1u, RLX_WG);
    } else lds_wait(smb + L_CTL + C_P1, 1);
#pragma unroll 1
    for (unsigned spin = 0; spin < SPIN_CAP; ++spin) {
      u32x4 pv[4], zv;
      FENCE();
#pragma unroll
      for (int r = 0; r < 4; ++r) pv[r] = __builtin_amdgcn_raw_buffer_load_b128(wsr, base + r * 1024, 0, 16);
      zv = __builtin_amdgcn_raw_buffer_load_b128(wsr, WS_ZPART + (hp * 16 + rb) * 1024 + lane * 16, 0, 16);
      bool ok = zv[1] == tlo && zv[3] == thi;
      s = (f32x4){}; z = (f32x2){UFLT(zv[0]), UFLT(zv[2])};
#pragma unroll
      for (int r = 0; r < 4; ++r) {
        ok = ok && (pv[r][1] >> 8) == ta && (pv[r][3] >> 8) == tb24;
        s[0] += dec20a(pv[r][0]); s[1] += dec20b(pv[r][0], pv[r][1]); s[2] += dec20a(pv[r][2]); s[3] += dec20b(pv[r][2], pv[r][3]);
      }
      if (ALL(ok)) break;
      __builtin_amdgcn_s_sleep(8);
    }
#pragma unroll
    for (int i = 0; i < 4; ++i) { s[i] += shxf(s[i], 16); s[i] += xswap(s[i]); }
    if (lane < 16) __builtin_amdgcn_raw_buffer_store_b128((u32x4){pk2(s[0], s[1]), tlo, pk2(s[2], s[3]), thi}, wsr, WS_KVF + ((hp * 16 + rb) * 64 + w4 * 16 + l15) * 16, 0, 16);
    if (w4 == 0) {
#pragma unroll
      for (int i = 0; i < 2; ++i) { z[i] += shxf(z[i], 4); z[i] += shxf(z[i], 8); z[i] += shxf(z[i], 16); z[i] += xswap(z[i]); }
      const unsigned zp = pk2(z[0], z[1]), zo = shx(zp, 1);
      if (lane < 4 && !(lane & 1)) __builtin_amdgcn_raw_buffer_store_b128((u32x4){zp, tlo, zo, thi}, wsr, WS_ZF + ((hp * 16 + rb) * 2 + (lane >> 1)) * 16, 0, 16);
    }
  }
  {
    bf16x8 kf[4], zf[4];
    if (w4 == 0) {
#pragma unroll 1
      for (unsigned sp = 0; sp < SPIN_CAP; ++sp) {
        FENCE();
        const u32x4 v = __builtin_amdgcn_raw_buffer_load_b128(wsr, WS_KVF + ((hp * 16 + (lane >> 2)) * 64 + (lane & 3) * 16 + 15) * 16, 0, 16);
        if (ALL(v[1] == tlo && v[3] == thi)) break;
        __builtin_amdgcn_s_sleep(PSLEEP);
      }
      if (lane == 0) __hip_atomic_store((LAS unsigned*)(smb + L_CTL + C_P2), 1u, RLX_WG);
    } else lds_wait(smb + L_CTL + C_P2, 1);
#pragma unroll 1
    for (unsigned spin = 0; spin < SPIN_CAP; ++spin) {
      u32x4 a[8], zz[8];
      FENCE();
#pragma unroll
      for (int j = 0; j < 8; ++j) {
        a[j] = __builtin_amdgcn_raw_buffer_load_b128(wsr, WS_KVF + ((hp * 16 + hi * 8 + j) * 64 + lane) * 16, 0, 16);
        zz[j] = __builtin_amdgcn_raw_buffer_load_b128(wsr, WS_ZF + ((hp * 16 + hi * 8 + j) * 2 + h) * 16, 0, 16);
      }
      bool ok = true;
#pragma unroll
      for (int j = 0; j < 8; ++j) ok = ok && a[j][1] == tlo && a[j][3] == thi && zz[j][1] == tlo && zz[j][3] == thi;
#pragma unroll
      for (int f = 0; f < 4; ++f) {
        kf[f] = __builtin_bit_cast(bf16x8, (u32x4){a[2 * f][0], a[2 * f][2], a[2 * f + 1][0], a[2 * f + 1][2]});
        zf[f] = __builtin_bit_cast(bf16x8, (u32x4){zz[2 * f][0], zz[2 * f][2], zz[2 * f + 1][0], zz[2 * f + 1][2]});
      }
      if (ALL(ok)) break;
      __builtin_amdgcn_s_sleep(8);
    }
#pragma unroll
    for (int tt = 0; tt < 2; ++tt) {
      f32x16 num = {}, den = {};
#pragma unroll
      for (int f = 0; f < 4; ++f) { num = MFMA32(kf[f], phq[tt][f], num); den = MFMA32(zf[f], phq[tt][f], den); }
      const float rd = 1.0f / rbf(rbf(den[0]) + 1e-6f);
      u32x4 o0, o1;
#pragma unroll
      for (int i = 0; i < 4; ++i) {
        const unsigned a0 = pk2(num[2 * i], num[2 * i + 1]), a1 = pk2(num[8 + 2 * i], num[9 + 2 * i]);
        o0[i] = pk2(blo(a0) * rd, bhi(a0) * rd); o1[i] = pk2(blo(a1) * rd, bhi(a1) * rd);
      }
      const unsigned dst = WS_OF + (((rb * 4 + mi * 2 + tt) * 32 + head) * 2) * 1024 + lane * 16;
      __builtin_amdgcn_raw_buffer_store_b128(o0, wsr, dst, 0, 16);
      __builtin_amdgcn_raw_buffer_store_b128(o1, wsr, dst + 1024, 0, 16);
    }
    set_flag(wsr, WS_F4 + (rb * 64 + hp * 4 + w4) * 8, tgt, lane);
    lds_signal(smb + L_CTL + C_OP, lane);
  }
  }
  {
    group_wait_flags(wsr, WS_F4 + rb2 * 512, tgt, lane, w4 == 0, smb + L_CTL + C_P4);
    const int hr = (idx & 7) * 4;
    bf16x8 of[20];
    const unsigned osrc = WS_OF + ((rb2 * 4 + w4) * 64) * 1024 + lane * 16;
#pragma unroll
    for (int i = 0; i < 10; ++i) {
      const unsigned o = osrc + ((i + hr) & 31) * 2048;
      of[2 * i] = __builtin_bit_cast(bf16x8, __builtin_amdgcn_raw_buffer_load_b128(wsr, o, 0, 16));
      of[2 * i + 1] = __builtin_bit_cast(bf16x8, __builtin_amdgcn_raw_buffer_load_b128(wsr, o + 1024, 0, 16));
    }
    f32x16 acc2[2] = {(f32x16){}, (f32x16){}};
    const unsigned wa0 = smb + (4 * h + q4) * 128 + (((2 * blk + (p4 >> 1)) ^ ((q4 >> 1) << 2)) << 4) + 8 * (p4 & 1);
    const unsigned wa1 = smb + (4 * h + q4) * 128 + (((4 + 2 * blk + (p4 >> 1)) ^ ((q4 >> 1) << 2)) << 4) + 8 * (p4 & 1);
    bf16x8 wfr[2][4];
    lds_wait(smb + L_CTL + C_OP, 4);
    lds_wait(smb + L_CTL + C_WO3, 4);
    { const unsigned ho = hr * 4096;
      wfr[0][0] = trfrag(wa0 + ho, 1024); wfr[0][1] = trfrag(wa1 + ho, 1024); wfr[0][2] = trfrag(wa0 + ho + 2048, 1024); wfr[0][3] = trfrag(wa1 + ho + 2048, 1024); }
#pragma unroll
    for (int i = 0; i < 32; ++i) {
      const int sl = i % 10, b = i & 1, n = b ^ 1;
      const unsigned ho = ((i + 1 + hr) & 31) * 4096;
      acc2[0] = MFMA32(wfr[b][0], of[2 * sl], acc2[0]); SCHED();
      if (i < 31) wfr[n][0] = trfrag(wa0 + ho, 1024);
      SCHED();
      acc2[1] = MFMA32(wfr[b][1], of[2 * sl], acc2[1]); SCHED();
      if (i < 31) wfr[n][1] = trfrag(wa1 + ho, 1024);
      SCHED();
      acc2[0] = MFMA32(wfr[b][2], of[2 * sl + 1], acc2[0]); SCHED();
      if (i < 31) wfr[n][2] = trfrag(wa0 + ho + 2048, 1024);
      SCHED();
      acc2[1] = MFMA32(wfr[b][3], of[2 * sl + 1], acc2[1]); SCHED();
      if (i < 31) wfr[n][3] = trfrag(wa1 + ho + 2048, 1024);
      if (i < 22) {
        const unsigned o = osrc + ((10 + i + hr) & 31) * 2048;
        of[2 * sl] = __builtin_bit_cast(bf16x8, __builtin_amdgcn_raw_buffer_load_b128(wsr, o, 0, 16));
        of[2 * sl + 1] = __builtin_bit_cast(bf16x8, __builtin_amdgcn_raw_buffer_load_b128(wsr, o + 1024, 0, 16));
      }
      SCHED();
    }
    const unsigned orow = ((rb2 * 128 + w4 * 32 + l31) * 2048 + 1024 + cb * 64) * 2;
#pragma unroll
    for (int c = 0; c < 2; ++c)
#pragma unroll
      for (int a = 0; a < 4; a += 2) {
        const unsigned x0 = pk2(acc2[c][4 * a], acc2[c][4 * a + 1]), x1 = pk2(acc2[c][4 * a + 2], acc2[c][4 * a + 3]);
        const unsigned y0 = pk2(acc2[c][4 * a + 4], acc2[c][4 * a + 5]), y1 = pk2(acc2[c][4 * a + 6], acc2[c][4 * a + 7]);
        const auto r0 = __builtin_amdgcn_permlane32_swap(x0, y0, false, false);
        const auto r1 = __builtin_amdgcn_permlane32_swap(x1, y1, false, false);
        __builtin_amdgcn_raw_buffer_store_b128((u32x4){r0[0], r1[0], r0[1], r1[1]}, outr, orow + (c * 32 + 8 * a + 8 * h) * 2, 0, 0);
      }
  }
}

#if defined(__HIP_DEVICE_COMPILE__)
#pragma clang attribute pop
#endif

extern "C" void kernel_launch(void* const* d_in, const int* in_sizes, int n_in, void* d_out, int out_size, void* d_ws, size_t ws_size, hipStream_t stream) {
  (void)hipFuncSetAttribute((const void*)fused, hipFuncAttributeMaxDynamicSharedMemorySize, LDS_TOTAL);
  Args a;
  a.q = (const u16*)d_in[0]; a.k = (const u16*)d_in[1]; a.v = (const u16*)d_in[2]; a.sk = (const u16*)d_in[3]; a.h = (const u16*)d_in[4]; a.dv = (const u16*)d_in[5];
  a.x = (const u16*)d_in[6]; a.wqkv = (const u16*)d_in[7]; a.wphi = (const u16*)d_in[8]; a.wo = (const u16*)d_in[9];
  a.out = (u16*)d_out; a.ws = (char*)d_ws;
  fused<<<256, 512, LDS_TOTAL, stream>>>(a);
}
```
